```python
import torch

def _softmax(x, axis=-1):
    return torch.softmax(x, dim=axis)
def _gelu(x):
    return torch.nn.functional.gelu(x, approximate="tanh")
def _silu(x):
    return torch.nn.functional.silu(x)
def _sigmoid(x):
    return torch.sigmoid(x)
def _layernorm(x, axis=-1, eps=1e-5):
    m = x.mean(dim=axis, keepdim=True)
    v = x.var(dim=axis, keepdim=True, unbiased=False)
    return (x - m) / torch.sqrt(v + eps)
def _scatter_add(tgt, idx, src, axis=0):
    return tgt.clone().index_add_(axis, idx, src)
def _segment_sum(src, seg, n):
    out = torch.zeros((n,) + src.shape[1:], dtype=src.dtype)
    return out.index_add_(0, seg, src)
def _pad(x, pw):
    flat = tuple(v for pair in reversed(pw) for v in pair)
    return torch.nn.functional.pad(x, flat)

kernel_name = 'interleave_layers__ssm_conv_s4__geglu_ffn__prompt'

def setup_inputs():

    _w = (lambda a: (lambda t: t.to(torch.bfloat16) if t.is_floating_point() else t)(torch.as_tensor(a)))
    def _A_setup():
        B, L, D, N = 1, 2048, 512, 128
        A_re = -(lambda _x: torch.exp(torch.as_tensor(_x)))((lambda *s: torch.randn(*s, dtype=torch.bfloat16) * 1.6)(D, N))
        A_im = torch.pi * (lambda *s: torch.randn(*s, dtype=torch.bfloat16) * 0.2)(D, N)
        B_re = (lambda *s: torch.randn(*s, dtype=torch.bfloat16) * 0.2)(D, N)
        B_im = (lambda *s: torch.randn(*s, dtype=torch.bfloat16) * 0.2)(D, N)
        C_re = (lambda *s: torch.randn(*s, dtype=torch.bfloat16) * 0.2)(D, N)
        C_im = (lambda *s: torch.randn(*s, dtype=torch.bfloat16) * 0.2)(D, N)
        log_dt = (lambda *s: torch.randn(*s, dtype=torch.bfloat16) * 0.2)(D) * 0.5 - 4.0
        Dskip = (lambda *s: torch.randn(*s, dtype=torch.bfloat16) * 0.2)(D)
        return {
            "u": _w((lambda *s: torch.randn(*s, dtype=torch.bfloat16) * 0.05)(B, L, D)),
            "A_re": _w(A_re),
            "A_im": _w(A_im),
            "B_re": _w(B_re),
            "B_im": _w(B_im),
            "C_re": _w(C_re),
            "C_im": _w(C_im),
            "log_dt": _w(log_dt),
            "Dskip": _w(Dskip),
        }
    _A_in = _A_setup()

    def _B_setup():
        B, S, D, F = 1, 2048, 512, 1792
        return {
            "x": _w((lambda *s: torch.randn(*s, dtype=torch.bfloat16) * 0.05)(B, S, D)),
            "w_gate": _w((lambda *s: torch.randn(*s, dtype=torch.bfloat16) * 0.05)(D, F)),
            "w_up": _w((lambda *s: torch.randn(*s, dtype=torch.bfloat16) * 0.05)(D, F)),
            "w_down": _w((lambda *s: torch.randn(*s, dtype=torch.bfloat16) * 0.05)(F, D)),
        }
    _B_in = _B_setup()

    return {
        "A_u": _A_in["u"],
        "A_A_re": _A_in["A_re"],
        "A_A_im": _A_in["A_im"],
        "A_B_re": _A_in["B_re"],
        "A_B_im": _A_in["B_im"],
        "A_C_re": _A_in["C_re"],
        "A_C_im": _A_in["C_im"],
        "A_log_dt": _A_in["log_dt"],
        "A_Dskip": _A_in["Dskip"],
        "B_x": _B_in["x"],
        "B_w_gate": _B_in["w_gate"],
        "B_w_up": _B_in["w_up"],
        "B_w_down": _B_in["w_down"],
    }

def reference(A_u, A_A_re, A_A_im, A_B_re, A_B_im, A_C_re, A_C_im, A_log_dt, A_Dskip, B_x, B_w_gate, B_w_up, B_w_down):
    def _A_ref(u, A_re, A_im, B_re, B_im, C_re, C_im, log_dt, Dskip):
        Bsz, L, D = u.shape
        dt = torch.exp(log_dt)[:, None]
        A = A_re + 1j * A_im
        Bc = B_re + 1j * B_im
        Cc = C_re + 1j * C_im
        logdA = dt * A
        dB = dt * Bc
        l = (lambda x, dt: x.to(dt))(torch.arange(L), A_re.dtype)
        P = torch.exp(logdA[:, :, None] * l[None, None, :])
        K = torch.einsum("dn,dnl,dn->dl", Cc, P, dB).real
        L2 = 2 * L
        Kp = _pad(K, ((0, 0), (0, L)))
        up = (lambda x, *d: x.permute(*d))(u, 0, 2, 1)
        up = _pad(up, ((0, 0), (0, 0), (0, L)))
        Kf = (lambda x, axes=(-2,-1): torch.fft.rfft2(x.float(), dim=axes))(Kp[:, None, :], axes=(-2, -1))
        Uf = (lambda x, axes=(-2,-1): torch.fft.rfft2(x.float(), dim=axes))(up[:, :, None, :], axes=(-2, -1))
        Yf = Uf * Kf[None, :, :, :]
        y = (lambda x, s, axes=(-2,-1): torch.fft.irfft2(x, s=s, dim=axes).to(torch.bfloat16))(Yf, (1, L2), axes=(-2, -1))[:, :, 0, :L]
        y = (lambda x, *d: x.permute(*d))(y, 0, 2, 1) + u * Dskip[None, None, :]
        return y

    def _B_ref(x, w_gate, w_up, w_down):
        return (_gelu(x @ w_gate) * (x @ w_up)) @ w_down

    x = A_u
    x = x + _A_ref(_layernorm(x, axis=-1), A_A_re, A_A_im, A_B_re, A_B_im, A_C_re, A_C_im, A_log_dt, A_Dskip)
    x = x + _B_ref(_layernorm(x, axis=-1), B_w_gate, B_w_up, B_w_down)
    x = x + _A_ref(_layernorm(x, axis=-1), A_A_re, A_A_im, A_B_re, A_B_im, A_C_re, A_C_im, A_log_dt, A_Dskip)
    x = x + _B_ref(_layernorm(x, axis=-1), B_w_gate, B_w_up, B_w_down)
    return x
```

```cpp
#include <hip/hip_runtime.h>
#include <hip/hip_bf16.h>
#include <stdint.h>

typedef unsigned short u16;
typedef __attribute__((ext_vector_type(8))) short bf16x8;
typedef __attribute__((ext_vector_type(4))) float f32x4;

#define LSEQ 2048
#define DM 512
#define NST 128
#define FF 1792

#define WS_PQ      0
#define WS_W1      (16u << 20)
#define WS_WD      (WS_W1 + (4u << 20))
#define WS_H       (WS_WD + (2u << 20))
#define WS_X1      (WS_H + (8u << 20))
#define WS_X2      (WS_X1 + (2u << 20))
#define WS_X3      (WS_X2 + (2u << 20))
#define WS_ST0     (WS_X3 + (2u << 20))
#define WS_ST2     (WS_ST0 + (256u << 10))
#define WS_ST4     (WS_ST2 + (256u << 10))

static __device__ __forceinline__ float bf2f(u16 v) { return __uint_as_float(((unsigned)v) << 16); }
static __device__ __forceinline__ u16 f2bf(float f) {
  __hip_bfloat16 b = __float2bfloat16(f);
  return *reinterpret_cast<u16*>(&b);
}
static __device__ __forceinline__ float rbf(float f) { return bf2f(f2bf(f)); }
static __device__ __forceinline__ unsigned pack2(float lo, float hi) {
  return (unsigned)f2bf(lo) | ((unsigned)f2bf(hi) << 16);
}

struct cf { float x, y; };
static __device__ __forceinline__ cf cmul(cf a, cf b) { return cf{a.x * b.x - a.y * b.y, a.x * b.y + a.y * b.x}; }
static __device__ __forceinline__ cf cmulc(cf a, cf b) { return cf{a.x * b.x + a.y * b.y, a.y * b.x - a.x * b.y}; }

template <int S>
static __device__ __forceinline__ void dft4(cf &a0, cf &a1, cf &a2, cf &a3) {
  cf t0{a0.x + a2.x, a0.y + a2.y}, t1{a0.x - a2.x, a0.y - a2.y};
  cf t2{a1.x + a3.x, a1.y + a3.y}, t3{a1.x - a3.x, a1.y - a3.y};
  cf r = (S < 0) ? cf{t3.y, -t3.x} : cf{-t3.y, t3.x};
  a0 = cf{t0.x + t2.x, t0.y + t2.y};
  a1 = cf{t1.x + r.x, t1.y + r.y};
  a2 = cf{t0.x - t2.x, t0.y - t2.y};
  a3 = cf{t1.x - r.x, t1.y - r.y};
}

template <int S>
static __device__ __forceinline__ void dft16(cf (&v)[16]) {
  const float C1 = 0.92387953251128674f, S1 = 0.38268343236508978f, R = 0.70710678118654752f;
#pragma unroll
  for (int b = 0; b < 4; ++b) dft4<S>(v[b], v[4 + b], v[8 + b], v[12 + b]);
  v[5] = cmul(v[5], cf{C1, S * S1});
  v[6] = cmul(v[6], cf{R, S * R});
  v[7] = cmul(v[7], cf{S1, S * C1});
  v[9] = cmul(v[9], cf{R, S * R});
  v[10] = (S < 0) ? cf{v[10].y, -v[10].x} : cf{-v[10].y, v[10].x};
  v[11] = cmul(v[11], cf{-R, S * R});
  v[13] = cmul(v[13], cf{S1, S * C1});
  v[14] = cmul(v[14], cf{-R, S * R});
  v[15] = cmul(v[15], cf{-C1, -S * S1});
#pragma unroll
  for (int c = 0; c < 4; ++c) dft4<S>(v[4 * c], v[4 * c + 1], v[4 * c + 2], v[4 * c + 3]);
  cf o[16];
#pragma unroll
  for (int k = 0; k < 16; ++k) o[k] = v[4 * (k & 3) + (k >> 2)];
#pragma unroll
  for (int k = 0; k < 16; ++k) v[k] = o[k];
}

#define XBUF 4368

static __device__ __forceinline__ void fft_fwd(cf (&v)[16], cf* bufA, cf* bufB, const cf* T256, const cf* T4096, int t) {
  const int lo = t & 15, hi = t >> 4;
  dft16<-1>(v);
#pragma unroll
  for (int k0 = 1; k0 < 16; ++k0) v[k0] = cmul(v[k0], T256[hi * k0]);
#pragma unroll
  for (int k0 = 0; k0 < 16; ++k0) bufA[272 * k0 + t] = v[k0];
  __syncthreads();
#pragma unroll
  for (int n1 = 0; n1 < 16; ++n1) v[n1] = bufA[272 * hi + 16 * n1 + lo];
  dft16<-1>(v);
  {
    cf b = T4096[lo * hi];
#pragma unroll
    for (int k1 = 0; k1 < 16; ++k1) v[k1] = cmul(v[k1], (k1 == 0) ? b : cmul(b, T256[lo * k1]));
  }
#pragma unroll
  for (int k1 = 0; k1 < 16; ++k1) bufB[272 * k1 + hi + 17 * lo] = v[k1];
  __syncthreads();
#pragma unroll
  for (int n0 = 0; n0 < 16; ++n0) v[n0] = bufB[272 * hi + lo + 17 * n0];
  dft16<-1>(v);
}

static __device__ __forceinline__ void fft_inv(cf (&v)[16], cf* bufA, cf* bufB, const cf* T256, const cf* T4096, int t) {
  const int lo = t & 15, hi = t >> 4;
  dft16<1>(v);
#pragma unroll
  for (int n0 = 1; n0 < 16; ++n0) v[n0] = cmulc(v[n0], T256[n0 * hi]);
#pragma unroll
  for (int n0 = 0; n0 < 16; ++n0) bufA[273 * n0 + t] = v[n0];
  __syncthreads();
#pragma unroll
  for (int k1 = 0; k1 < 16; ++k1) v[k1] = bufA[273 * lo + hi + 16 * k1];
  dft16<1>(v);
  {
    cf b = T4096[hi * lo];
#pragma unroll
    for (int n1 = 0; n1 < 16; ++n1) v[n1] = cmulc(v[n1], (n1 == 0) ? b : cmul(b, T256[hi * n1]));
  }
#pragma unroll
  for (int n1 = 0; n1 < 16; ++n1) bufB[272 * n1 + t] = v[n1];
  __syncthreads();
#pragma unroll
  for (int k0 = 0; k0 < 16; ++k0) v[k0] = bufB[272 * hi + 16 * k0 + lo];
  dft16<1>(v);
}

static __device__ __forceinline__ void init_twiddles(cf* T256, cf* T4096, int t) {
  float s, c;
  sincospif(-(float)t * (1.0f / 128.0f), &s, &c);
  T256[t] = cf{c, s};
  sincospif(-(float)t * (1.0f / 2048.0f), &s, &c);
  T4096[t] = cf{c, s};
}

static __device__ __forceinline__ cf cexp_l(float re, float im, float l) {
  float e = expf(re * l);
  float s, c;
  sincosf(im * l, &s, &c);
  return cf{e * c, e * s};
}

#define PREP_KGEN 256
#define PREP_W1   896
#define PREP_WD   448
#define PREP_ST   64
#define PREP_GRID (PREP_KGEN + PREP_W1 + PREP_WD + PREP_ST)

#define TZS 65
#define TWS 45

__global__ __launch_bounds__(256) void prep_kernel(
    const u16* __restrict__ Are, const u16* __restrict__ Aim, const u16* __restrict__ Bre, const u16* __restrict__ Bim,
    const u16* __restrict__ Cre, const u16* __restrict__ Cim, const u16* __restrict__ logdt,
    const u16* __restrict__ x0, const u16* __restrict__ Wg, const u16* __restrict__ Wu, const u16* __restrict__ Wd,
    float4* __restrict__ PQ, u16* __restrict__ W1f, u16* __restrict__ Wdf, float2* __restrict__ st0) {
  extern __shared__ __attribute__((aligned(16))) char smem[];
  const int t = threadIdx.x;
  int bid = blockIdx.x;
  if (bid >= PREP_KGEN) {
    bid -= PREP_KGEN;
    if (bid < PREP_W1) {
      int u = bid * 256 + t;
      int lane = u & 63, nt = (u >> 6) % 14, r = (u >> 6) / 14, ks = r & 15, cb = r >> 4;
      const u16* W = (nt < 7) ? Wg : Wu;
      int f = cb * 112 + (nt % 7) * 16 + (lane & 15);
      int k = ks * 32 + (lane >> 4) * 8;
      unsigned wv[4];
#pragma unroll
      for (int i = 0; i < 4; ++i)
        wv[i] = (unsigned)W[(size_t)(k + 2 * i) * FF + f] | ((unsigned)W[(size_t)(k + 2 * i + 1) * FF + f] << 16);
      *reinterpret_cast<uint4*>(W1f + (size_t)u * 8) = make_uint4(wv[0], wv[1], wv[2], wv[3]);
      return;
    }
    bid -= PREP_W1;
    if (bid < PREP_WD) {
      int u = bid * 256 + t;
      int lane = u & 63, nt = (u >> 6) & 3, r = u >> 8, ks = r % 56, nb = r / 56;
      int n = nb * 64 + nt * 16 + (lane & 15);
      int k = ks * 32 + (lane >> 4) * 8;
      unsigned wv[4];
#pragma unroll
      for (int i = 0; i < 4; ++i)
        wv[i] = (unsigned)Wd[(size_t)(k + 2 * i) * DM + n] | ((unsigned)Wd[(size_t)(k + 2 * i + 1) * DM + n] << 16);
      *reinterpret_cast<uint4*>(Wdf + (size_t)u * 8) = make_uint4(wv[0], wv[1], wv[2], wv[3]);
      return;
    }
    bid -= PREP_WD;
    {
      int wave = t >> 6, lane = t & 63;
      for (int i = 0; i < 8; ++i) {
        int row = (bid * 4 + wave) * 8 + i;
        uint4 q = *reinterpret_cast<const uint4*>(x0 + (size_t)row * DM + lane * 8);
        unsigned w[4] = {q.x, q.y, q.z, q.w};
        float s = 0.f, ss = 0.f;
#pragma unroll
        for (int j = 0; j < 4; ++j) {
          float a = bf2f((u16)(w[j] & 0xffff)), b = bf2f((u16)(w[j] >> 16));
          s += a + b; ss += a * a + b * b;
        }
#pragma unroll
        for (int o = 32; o > 0; o >>= 1) { s += __shfl_xor(s, o); ss += __shfl_xor(ss, o); }
        if (lane < 8) st0[row * 8 + lane] = (lane == 0) ? make_float2(s, ss) : make_float2(0.f, 0.f);
      }
      return;
    }
  }

  const int p = bid;
  cf* TZ = reinterpret_cast<cf*>(smem);
  cf* TW = TZ + 128 * TZS;
  float* red = reinterpret_cast<float*>(TW + 128 * TWS);
  float* Kv = red + 8 * 704;
  cf* T256 = reinterpret_cast<cf*>(Kv + 2 * 704);
  cf* T4096 = T256 + 256;
  init_twiddles(T256, T4096, t);

  for (int ch = 0; ch < 2; ++ch) {
    const int d = 2 * p + ch;
    {
      const int n = t & 127;
      const float dt = rbf(expf(bf2f(logdt[d])));
      const float re = dt * bf2f(Are[d * NST + n]);
      const float im = dt * bf2f(Aim[d * NST + n]);
      if (t < 128) {
        cf z1 = cexp_l(re, im, 1.0f);
        cf zs = z1;
#pragma unroll
        for (int s = 0; s < 4; ++s) {
          cf acc{1.f, 0.f};
          TZ[n * TZS + s * 16] = acc;
#pragma unroll
          for (int b = 1; b < 16; ++b) { acc = cmul(acc, zs); TZ[n * TZS + s * 16 + b] = acc; }
          zs = cmul(zs, zs);
        }
      } else {
        cf dB{dt * bf2f(Bre[d * NST + n]), dt * bf2f(Bim[d * NST + n])};
        cf Cc{bf2f(Cre[d * NST + n]), bf2f(Cim[d * NST + n])};
        cf c = cmul(Cc, dB);
        cf z16 = cexp_l(re, im, 16.0f);
        cf z256 = cexp_l(re, im, 256.0f);
        cf acc = c;
        TW[n * TWS + 0] = acc;
#pragma unroll
        for (int a = 1; a < 16; ++a) { acc = cmul(acc, z16); TW[n * TWS + a] = acc; }
        cf zs16 = cmul(z16, z16);
        cf zs128 = z256;
#pragma unroll
        for (int q = 0; q < 3; ++q) {
          acc = cmul(c, zs128);
          TW[n * TWS + 16 + 8 * q] = acc;
#pragma unroll
          for (int a = 1; a < 8; ++a) { acc = cmul(acc, zs16); TW[n * TWS + 16 + 8 * q + a] = acc; }
          if (q == 2) { acc = cmul(acc, zs16); TW[n * TWS + 40] = acc; }
          zs16 = cmul(zs16, zs16);
          zs128 = cmul(zs128, zs128);
        }
        TW[n * TWS + 41] = cf{0.f, 0.f};
        TW[n * TWS + 42] = cf{0.f, 0.f};
        TW[n * TWS + 43] = cf{0.f, 0.f};
      }
    }
    __syncthreads();
    {
      const int g = t >> 4, mg = t & 15;
      float acc[4][16];
#pragma unroll
      for (int r = 0; r < 4; ++r)
#pragma unroll
        for (int b = 0; b < 16; ++b) acc[r][b] = 0.f;
      if (g < 11) {
        const int s = (g < 4) ? 0 : ((g < 6) ? 1 : ((g < 8) ? 2 : 3));
        for (int i = 0; i < 8; ++i) {
          const int n = mg + 16 * i;
          cf w[4];
#pragma unroll
          for (int r = 0; r < 4; ++r) w[r] = TW[n * TWS + 4 * g + r];
#pragma unroll
          for (int b = 0; b < 16; ++b) {
            cf z = TZ[n * TZS + s * 16 + b];
#pragma unroll
            for (int r = 0; r < 4; ++r) acc[r][b] += w[r].x * z.x - w[r].y * z.y;
          }
        }
        if (mg >= 8) {
#pragma unroll
          for (int r = 0; r < 4; ++r)
#pragma unroll
            for (int b = 0; b < 16; ++b) red[(mg - 8) * 704 + (4 * g + r) * 16 + b] = acc[r][b];
        }
      }
      __syncthreads();
      if (g < 11 && mg < 8) {
#pragma unroll
        for (int r = 0; r < 4; ++r)
#pragma unroll
          for (int b = 0; b < 16; ++b) red[mg * 704 + (4 * g + r) * 16 + b] += acc[r][b];
      }
    }
    __syncthreads();
    for (int o = t; o < 704; o += 256) {
      float s = 0.f;
#pragma unroll
      for (int mg = 0; mg < 8; ++mg) s += red[mg * 704 + o];
      Kv[ch * 704 + o] = s;
    }
    __syncthreads();
  }

  cf* bufA = reinterpret_cast<cf*>(smem);
  cf* bufB = bufA + XBUF;
  cf v[16];
#pragma unroll
  for (int j = 0; j < 8; ++j) {
    int l = 256 * j + t;
    unsigned ub = __float_as_uint((float)l);
    ub = (ub + 0x7fffu + ((ub >> 16) & 1u)) & 0xffff0000u;
    int lb = (int)__uint_as_float(ub);
    int row, b;
    if (lb < 256) { row = lb >> 4; b = lb & 15; }
    else {
      int q = (lb < 512) ? 0 : ((lb < 1024) ? 1 : 2);
      int vv = lb >> (q + 1);
      row = 16 + 8 * q + ((vv >> 4) - 8); b = vv & 15;
    }
    v[j] = cf{Kv[row * 16 + b], Kv[704 + row * 16 + b]};
  }
#pragma unroll
  for (int j = 8; j < 16; ++j) v[j] = cf{0.f, 0.f};
  __syncthreads();
  fft_fwd(v, bufA, bufB, T256, T4096, t);
#pragma unroll
  for (int k2 = 0; k2 < 16; ++k2) bufA[t + 256 * k2] = v[k2];
  __syncthreads();
  const float sc = 0.25f / 4096.0f;
#pragma unroll
  for (int k2 = 0; k2 < 16; ++k2) {
    int k = t + 256 * k2;
    cf g = v[k2];
    cf m = bufA[(4096 - k) & 4095];
    cf h{m.x, -m.y};
    cf P{(g.x + g.y) + (h.x - h.y), (g.y - g.x) + (h.y + h.x)};
    cf Q{(g.x - g.y) + (h.x + h.y), (g.y + g.x) + (h.y - h.x)};
    PQ[((size_t)p * 16 + k2) * 256 + t] = make_float4(P.x * sc, P.y * sc, Q.x * sc, Q.y * sc);
  }
}

__global__ __launch_bounds__(256) void conv_kernel(const u16* __restrict__ xin, const float2* __restrict__ stats,
                                                   const float4* __restrict__ PQ, const u16* __restrict__ Dskip,
                                                   u16* __restrict__ xout) {
  __shared__ cf bufA[XBUF];
  __shared__ cf bufB[XBUF];
  __shared__ cf T256[256];
  __shared__ cf T4096[256];
  const int t = threadIdx.x, p = blockIdx.x;
  init_twiddles(T256, T4096, t);
  unsigned xr[8];
#pragma unroll
  for (int j = 0; j < 8; ++j) xr[j] = *reinterpret_cast<const unsigned*>(xin + (size_t)(256 * j + t) * DM + 2 * p);
  cf v[16];
  unsigned ur[8];
#pragma unroll
  for (int j = 0; j < 8; ++j) {
    const float4* sp = reinterpret_cast<const float4*>(stats + (size_t)(256 * j + t) * 8);
    float4 a = sp[0], b = sp[1], c = sp[2], d = sp[3];
    float s = ((a.x + a.z) + (b.x + b.z)) + ((c.x + c.z) + (d.x + d.z));
    float ss = ((a.y + a.w) + (b.y + b.w)) + ((c.y + c.w) + (d.y + d.w));
    float mean = s * (1.0f / DM);
    float var = fmaxf(ss * (1.0f / DM) - mean * mean, 0.f);
    float m = rbf(mean);
    float sd = rbf(sqrtf(rbf(rbf(var) + 1e-5f)));
    float xa = bf2f((u16)(xr[j] & 0xffff)), xb = bf2f((u16)(xr[j] >> 16));
    float ua = rbf(rbf(xa - m) / sd), ub = rbf(rbf(xb - m) / sd);
    v[j] = cf{ua, ub};
    ur[j] = pack2(ua, ub);
  }
#pragma unroll
  for (int j = 8; j < 16; ++j) v[j] = cf{0.f, 0.f};
  __syncthreads();
  fft_fwd(v, bufA, bufB, T256, T4096, t);
#pragma unroll
  for (int k2 = 0; k2 < 16; ++k2) bufA[t + 256 * k2] = v[k2];
  __syncthreads();
#pragma unroll
  for (int k2 = 0; k2 < 16; ++k2) {
    int k = t + 256 * k2;
    cf zc = bufA[(4096 - k) & 4095];
    zc.y = -zc.y;
    float4 pq = PQ[((size_t)p * 16 + k2) * 256 + t];
    cf a = cmul(v[k2], cf{pq.x, pq.y});
    cf b = cmul(zc, cf{pq.z, pq.w});
    v[k2] = cf{a.x + b.x, a.y + b.y};
  }
  fft_inv(v, bufB, bufA, T256, T4096, t);
  const float Da = bf2f(Dskip[2 * p]), Db = bf2f(Dskip[2 * p + 1]);
#pragma unroll
  for (int j = 0; j < 8; ++j) {
    float xa = bf2f((u16)(xr[j] & 0xffff)), xb = bf2f((u16)(xr[j] >> 16));
    float ua = bf2f((u16)(ur[j] & 0xffff)), ub = bf2f((u16)(ur[j] >> 16));
    float ya = rbf(rbf(v[j].x) + rbf(ua * Da));
    float yb = rbf(rbf(v[j].y) + rbf(ub * Db));
    *reinterpret_cast<unsigned*>(xout + (size_t)(256 * j + t) * DM + 2 * p) = pack2(xa + ya, xb + yb);
  }
}

static __device__ __forceinline__ float gelu_tanh(float x) {
  float inner = 0.7978845608028654f * (x + 0.044715f * x * x * x);
  return 0.5f * x * (1.0f + tanhf(inner));
}

#define FA_NS 4
#define FA_STAGE_BYTES (28 * 1024)

__global__ __launch_bounds__(256) void ffn_a_kernel(const u16* __restrict__ xin, const u16* __restrict__ W1f,
                                                    u16* __restrict__ hf) {
  extern __shared__ __attribute__((aligned(16))) char smem[];
  const int t = threadIdx.x, wave = t >> 6, lane = t & 63;
  const int bid = blockIdx.x;
  const int xcd = bid & 7, idx = bid >> 3;
  const int mblk = (xcd & 1) * 8 + (idx & 7);
  const int cb = (xcd >> 1) * 4 + (idx >> 3);
  const char* wsrc = reinterpret_cast<const char*>(W1f) + (size_t)cb * (16 * 14 * 1024);

#define FA_ISSUE(s)                                                                                   \
  do {                                                                                                \
    _Pragma("unroll") for (int i = 0; i < 7; ++i) {                                                  \
      const char* g = wsrc + (size_t)(s) * FA_STAGE_BYTES + (wave * 7 + i) * 1024 + lane * 16;        \
      char* l = smem + ((s) % FA_NS) * FA_STAGE_BYTES + (wave * 7 + i) * 1024;                        \
      __builtin_amdgcn_global_load_lds(reinterpret_cast<const unsigned*>(g),                          \
                                       reinterpret_cast<unsigned*>(l), 16, 0, 0);                     \
    }                                                                                                 \
  } while (0)
  FA_ISSUE(0); FA_ISSUE(1); FA_ISSUE(2);

  bf16x8 af[2][16];
  const int rbase = mblk * 128 + wave * 32 + (lane & 15);
#pragma unroll
  for (int mt = 0; mt < 2; ++mt)
#pragma unroll
    for (int ks = 0; ks < 16; ++ks)
      af[mt][ks] = *reinterpret_cast<const bf16x8*>(xin + (size_t)(rbase + mt * 16) * DM + ks * 32 + (lane >> 4) * 8);
#pragma unroll
  for (int mt = 0; mt < 2; ++mt) {
    float s = 0.f;
#pragma unroll
    for (int ks = 0; ks < 16; ++ks)
#pragma unroll
      for (int i = 0; i < 8; ++i) s += bf2f((u16)af[mt][ks][i]);
    s += __shfl_xor(s, 16); s += __shfl_xor(s, 32);
    float mean = s * (1.0f / DM);
    float q = 0.f;
#pragma unroll
    for (int ks = 0; ks < 16; ++ks)
#pragma unroll
      for (int i = 0; i < 8; ++i) { float dlt = bf2f((u16)af[mt][ks][i]) - mean; q += dlt * dlt; }
    q += __shfl_xor(q, 16); q += __shfl_xor(q, 32);
    float var = q * (1.0f / DM);
    float m = rbf(mean);
    float sd = rbf(sqrtf(rbf(rbf(var) + 1e-5f)));
    float rs = 1.0f / sd;
#pragma unroll
    for (int ks = 0; ks < 16; ++ks)
#pragma unroll
      for (int i = 0; i < 8; ++i) {
        float dd = rbf(bf2f((u16)af[mt][ks][i]) - m);
        af[mt][ks][i] = (short)f2bf(dd * rs);
      }
  }

  f32x4 acc[14][2];
#pragma unroll
  for (int nt = 0; nt < 14; ++nt) { acc[nt][0] = f32x4{0.f, 0.f, 0.f, 0.f}; acc[nt][1] = f32x4{0.f, 0.f, 0.f, 0.f}; }

#pragma unroll
  for (int it = 0; it < 8; ++it) {
    if (it <= 5) asm volatile("s_waitcnt vmcnt(14)" ::: "memory");
    else if (it == 6) asm volatile("s_waitcnt vmcnt(7)" ::: "memory");
    else asm volatile("s_waitcnt vmcnt(0)" ::: "memory");
    __builtin_amdgcn_s_barrier();
    if (it + 3 < 8) FA_ISSUE(it + 3);
    const char* sb = smem + (it % FA_NS) * FA_STAGE_BYTES + lane * 16;
#pragma unroll
    for (int k2 = 0; k2 < 2; ++k2) {
#pragma unroll
      for (int nt = 0; nt < 14; ++nt) {
        bf16x8 bfr = *reinterpret_cast<const bf16x8*>(sb + (k2 * 14 + nt) * 1024);
        acc[nt][0] = __builtin_amdgcn_mfma_f32_16x16x32_bf16(bfr, af[0][2 * it + k2], acc[nt][0], 0, 0, 0);
        acc[nt][1] = __builtin_amdgcn_mfma_f32_16x16x32_bf16(bfr, af[1][2 * it + k2], acc[nt][1], 0, 0, 0);
      }
    }
  }
#pragma unroll
  for (int mt = 0; mt < 2; ++mt) {
    const int mtg = mblk * 8 + wave * 2 + mt;
#pragma unroll
    for (int nt = 0; nt < 7; ++nt) {
      float hv[4];
#pragma unroll
      for (int j = 0; j < 4; ++j) {
        float g = rbf(acc[nt][mt][j]);
        float u = rbf(acc[nt + 7][mt][j]);
        hv[j] = rbf(gelu_tanh(g)) * u;
      }
      const int f = cb * 112 + nt * 16 + (lane >> 4) * 4;
      const int ks2 = f >> 5, kq = (f >> 3) & 3;
      size_t off = ((size_t)(mtg * 56 + ks2) * 64 + (lane & 15) + 16 * kq) * 8 + ((lane >> 4) & 1) * 4;
      *reinterpret_cast<uint2*>(hf + off) = make_uint2(pack2(hv[0], hv[1]), pack2(hv[2], hv[3]));
    }
  }
}

__global__ __launch_bounds__(256) void ffn_b_kernel(const u16* __restrict__ hf, const u16* __restrict__ Wdf,
                                                    const u16* __restrict__ xres, u16* __restrict__ xout,
                                                    float2* __restrict__ stout) {
  __shared__ float red[4][64][64];
  const int t = threadIdx.x, wave = t >> 6, lane = t & 63;
  const int bid = blockIdx.x;
  const int xcd = bid & 7, idx = bid >> 3;
  const int nb = xcd;
  const int mb = idx;
  f32x4 acc[4][4];
#pragma unroll
  for (int a = 0; a < 4; ++a)
#pragma unroll
    for (int b = 0; b < 4; ++b) acc[a][b] = f32x4{0.f, 0.f, 0.f, 0.f};
  const bf16x8* wp = reinterpret_cast<const bf16x8*>(Wdf) + ((size_t)nb * 56 * 4) * 64 + lane;
  const bf16x8* hp = reinterpret_cast<const bf16x8*>(hf) + ((size_t)mb * 4 * 56) * 64 + lane;
  const int k0 = wave * 14;
  bf16x8 wf[2][4], hfr[2][4];
#pragma unroll
  for (int i = 0; i < 4; ++i) {
    wf[0][i] = wp[((size_t)k0 * 4 + i) * 64];
    hfr[0][i] = hp[((size_t)i * 56 + k0) * 64];
  }
#pragma unroll
  for (int ks = 0; ks < 14; ++ks) {
    const int cur = ks & 1, nxt = cur ^ 1;
    if (ks + 1 < 14) {
#pragma unroll
      for (int i = 0; i < 4; ++i) {
        wf[nxt][i] = wp[((size_t)(k0 + ks + 1) * 4 + i) * 64];
        hfr[nxt][i] = hp[((size_t)i * 56 + k0 + ks + 1) * 64];
      }
    }
#pragma unroll
    for (int nt = 0; nt < 4; ++nt)
#pragma unroll
      for (int mt = 0; mt < 4; ++mt)
        acc[nt][mt] = __builtin_amdgcn_mfma_f32_16x16x32_bf16(wf[cur][nt], hfr[cur][mt], acc[nt][mt], 0, 0, 0);
  }
#pragma unroll
  for (int nt = 0; nt < 4; ++nt)
#pragma unroll
    for (int mt = 0; mt < 4; ++mt)
#pragma unroll
      for (int j = 0; j < 4; ++j) red[wave][(nt * 4 + mt) * 4 + j][lane] = acc[nt][mt][j];
  __syncthreads();
  const int row = mb * 64 + wave * 16 + (lane & 15);
  float s = 0.f, ss = 0.f;
#pragma unroll
  for (int nt = 0; nt < 4; ++nt) {
    float o[4];
#pragma unroll
    for (int j = 0; j < 4; ++j) {
      const int r = (nt * 4 + wave) * 4 + j;
      o[j] = (red[0][r][lane] + red[1][r][lane]) + (red[2][r][lane] + red[3][r][lane]);
    }
    const int n = nb * 64 + nt * 16 + (lane >> 4) * 4;
    uint2 xr = *reinterpret_cast<const uint2*>(xres + (size_t)row * DM + n);
    float x0 = bf2f((u16)(xr.x & 0xffff)) + rbf(o[0]);
    float x1 = bf2f((u16)(xr.x >> 16)) + rbf(o[1]);
    float x2 = bf2f((u16)(xr.y & 0xffff)) + rbf(o[2]);
    float x3 = bf2f((u16)(xr.y >> 16)) + rbf(o[3]);
    x0 = rbf(x0); x1 = rbf(x1); x2 = rbf(x2); x3 = rbf(x3);
    s += (x0 + x1) + (x2 + x3);
    ss += (x0 * x0 + x1 * x1) + (x2 * x2 + x3 * x3);
    *reinterpret_cast<uint2*>(xout + (size_t)row * DM + n) = make_uint2(pack2(x0, x1), pack2(x2, x3));
  }
  s += __shfl_xor(s, 16); s += __shfl_xor(s, 32);
  ss += __shfl_xor(ss, 16); ss += __shfl_xor(ss, 32);
  if (lane < 16) stout[(size_t)row * 8 + nb] = make_float2(s, ss);
}

extern "C" void kernel_launch(void* const* d_in, const int* in_sizes, int n_in,
                              void* d_out, int out_size, void* d_ws, size_t ws_size,
                              hipStream_t stream) {
  const u16* x0 = (const u16*)d_in[0];
  const u16* Are = (const u16*)d_in[1];
  const u16* Aim = (const u16*)d_in[2];
  const u16* Bre = (const u16*)d_in[3];
  const u16* Bim = (const u16*)d_in[4];
  const u16* Cre = (const u16*)d_in[5];
  const u16* Cim = (const u16*)d_in[6];
  const u16* logdt = (const u16*)d_in[7];
  const u16* Dskip = (const u16*)d_in[8];
  const u16* Wg = (const u16*)d_in[10];
  const u16* Wu = (const u16*)d_in[11];
  const u16* Wd = (const u16*)d_in[12];
  char* ws = (char*)d_ws;
  float4* PQ = (float4*)(ws + WS_PQ);
  u16* W1f = (u16*)(ws + WS_W1);
  u16* Wdf = (u16*)(ws + WS_WD);
  u16* hf = (u16*)(ws + WS_H);
  u16* x1 = (u16*)(ws + WS_X1);
  u16* x2 = (u16*)(ws + WS_X2);
  u16* x3 = (u16*)(ws + WS_X3);
  float2* st0 = (float2*)(ws + WS_ST0);
  float2* st2 = (float2*)(ws + WS_ST2);
  float2* st4 = (float2*)(ws + WS_ST4);
  u16* out = (u16*)d_out;

  const int prep_lds = 128 * TZS * 8 + 128 * TWS * 8 + 8 * 704 * 4 + 2 * 704 * 4 + 512 * 8;
  hipFuncSetAttribute((const void*)prep_kernel, hipFuncAttributeMaxDynamicSharedMemorySize, prep_lds);
  hipFuncSetAttribute((const void*)ffn_a_kernel, hipFuncAttributeMaxDynamicSharedMemorySize, FA_NS * FA_STAGE_BYTES);

  prep_kernel<<<PREP_GRID, 256, prep_lds, stream>>>(Are, Aim, Bre, Bim, Cre, Cim, logdt, x0, Wg, Wu, Wd, PQ, W1f, Wdf, st0);
  conv_kernel<<<256, 256, 0, stream>>>(x0, st0, PQ, Dskip, x1);
  ffn_a_kernel<<<256, 256, FA_NS * FA_STAGE_BYTES, stream>>>(x1, W1f, hf);
  ffn_b_kernel<<<256, 256, 0, stream>>>(hf, Wdf, x1, x2, st2);
  conv_kernel<<<256, 256, 0, stream>>>(x2, st2, PQ, Dskip, x3);
  ffn_a_kernel<<<256, 256, FA_NS * FA_STAGE_BYTES, stream>>>(x3, W1f, hf);
  ffn_b_kernel<<<256, 256, 0, stream>>>(hf, Wdf, x3, out, st4);
}
```

```cpp
#include <hip/hip_runtime.h>
#include <hip/hip_bf16.h>
#include <stdint.h>

typedef unsigned short u16;
typedef __attribute__((ext_vector_type(8))) short bf16x8;
typedef __attribute__((ext_vector_type(4))) float f32x4;

#define LSEQ 2048
#define DM 512
#define NST 128
#define FF 1792

#define WS_PQ      0
#define WS_W1      (16u << 20)
#define WS_WD      (WS_W1 + (4u << 20))
#define WS_H       (WS_WD + (2u << 20))
#define WS_X1      (WS_H + (8u << 20))
#define WS_X2      (WS_X1 + (2u << 20))
#define WS_X3      (WS_X2 + (2u << 20))
#define WS_ST0     (WS_X3 + (2u << 20))
#define WS_CS      (WS_ST0 + (64u << 10))

static __device__ __forceinline__ float bf2f(u16 v) { return __uint_as_float(((unsigned)v) << 16); }
static __device__ __forceinline__ u16 f2bf(float f) {
  __hip_bfloat16 b = __float2bfloat16(f);
  return *reinterpret_cast<u16*>(&b);
}
static __device__ __forceinline__ float rbf(float f) { return bf2f(f2bf(f)); }
static __device__ __forceinline__ unsigned pack2(float lo, float hi) {
  return (unsigned)f2bf(lo) | ((unsigned)f2bf(hi) << 16);
}

struct cf { float x, y; };
static __device__ __forceinline__ cf cmul(cf a, cf b) { return cf{a.x * b.x - a.y * b.y, a.x * b.y + a.y * b.x}; }
static __device__ __forceinline__ cf cmulc(cf a, cf b) { return cf{a.x * b.x + a.y * b.y, a.y * b.x - a.x * b.y}; }

template <int S>
static __device__ __forceinline__ void dft4(cf &a0, cf &a1, cf &a2, cf &a3) {
  cf t0{a0.x + a2.x, a0.y + a2.y}, t1{a0.x - a2.x, a0.y - a2.y};
  cf t2{a1.x + a3.x, a1.y + a3.y}, t3{a1.x - a3.x, a1.y - a3.y};
  cf r = (S < 0) ? cf{t3.y, -t3.x} : cf{-t3.y, t3.x};
  a0 = cf{t0.x + t2.x, t0.y + t2.y};
  a1 = cf{t1.x + r.x, t1.y + r.y};
  a2 = cf{t0.x - t2.x, t0.y - t2.y};
  a3 = cf{t1.x - r.x, t1.y - r.y};
}

template <int S>
static __device__ __forceinline__ void dft8(cf (&v)[8]) {
  const float R = 0.70710678118654752f;
  dft4<S>(v[0], v[2], v[4], v[6]);
  dft4<S>(v[1], v[3], v[5], v[7]);
  cf o1 = cmul(v[3], cf{R, S * R});
  cf o2 = (S < 0) ? cf{v[5].y, -v[5].x} : cf{-v[5].y, v[5].x};
  cf o3 = cmul(v[7], cf{-R, S * R});
  cf e0 = v[0], e1 = v[2], e2 = v[4], e3 = v[6], o0 = v[1];
  v[0] = cf{e0.x + o0.x, e0.y + o0.y}; v[4] = cf{e0.x - o0.x, e0.y - o0.y};
  v[1] = cf{e1.x + o1.x, e1.y + o1.y}; v[5] = cf{e1.x - o1.x, e1.y - o1.y};
  v[2] = cf{e2.x + o2.x, e2.y + o2.y}; v[6] = cf{e2.x - o2.x, e2.y - o2.y};
  v[3] = cf{e3.x + o3.x, e3.y + o3.y}; v[7] = cf{e3.x - o3.x, e3.y - o3.y};
}

#define XBUF 4608
#define NT 512

static __device__ __forceinline__ void fft_fwd(cf (&v)[8], cf* bA, cf* bB, const cf* TW, int t) {
  const int a0 = t & 7, a1 = (t >> 3) & 7, a2 = t >> 6;
  dft8<-1>(v);
#pragma unroll
  for (int k = 1; k < 8; ++k) v[k] = cmul(v[k], TW[k * t]);
#pragma unroll
  for (int k = 0; k < 8; ++k) bA[512 * k + t] = v[k];
  __syncthreads();
#pragma unroll
  for (int i = 0; i < 8; ++i) v[i] = bA[512 * a2 + (t & 63) + 64 * i];
  dft8<-1>(v);
#pragma unroll
  for (int k = 1; k < 8; ++k) v[k] = cmul(v[k], TW[8 * k * (t & 63)]);
#pragma unroll
  for (int k = 0; k < 8; ++k) bB[576 * k + (t & 63) + 72 * a2] = v[k];
  __syncthreads();
#pragma unroll
  for (int i = 0; i < 8; ++i) v[i] = bB[576 * a2 + a0 + 8 * i + 72 * a1];
  dft8<-1>(v);
#pragma unroll
  for (int k = 1; k < 8; ++k) v[k] = cmul(v[k], TW[64 * k * a0]);
#pragma unroll
  for (int k = 0; k < 8; ++k) bA[576 * k + 72 * a2 + 9 * a1 + a0] = v[k];
  __syncthreads();
#pragma unroll
  for (int i = 0; i < 8; ++i) v[i] = bA[576 * a2 + 72 * a1 + 9 * a0 + i];
  dft8<-1>(v);
}

static __device__ __forceinline__ void fft_inv(cf (&v)[8], cf* bA, cf* bB, const cf* TW, int t) {
  const int a0 = t & 7, a1 = (t >> 3) & 7, a2 = t >> 6;
  dft8<1>(v);
#pragma unroll
  for (int n = 1; n < 8; ++n) v[n] = cmulc(v[n], TW[64 * n * a2]);
#pragma unroll
  for (int n = 0; n < 8; ++n) bA[576 * a2 + 72 * a1 + 9 * a0 + n] = v[n];
  __syncthreads();
#pragma unroll
  for (int i = 0; i < 8; ++i) v[i] = bA[576 * i + 72 * a2 + 9 * a1 + a0];
  dft8<1>(v);
#pragma unroll
  for (int n = 0; n < 8; ++n) v[n] = cmulc(v[n], TW[8 * a2 * (a0 + 8 * n)]);
#pragma unroll
  for (int n = 0; n < 8; ++n) bB[576 * a2 + a0 + 8 * n + 72 * a1] = v[n];
  __syncthreads();
#pragma unroll
  for (int i = 0; i < 8; ++i) v[i] = bB[576 * i + (t & 63) + 72 * a2];
  dft8<1>(v);
#pragma unroll
  for (int n = 0; n < 8; ++n) v[n] = cmulc(v[n], TW[a2 * ((t & 63) + 64 * n)]);
#pragma unroll
  for (int n = 0; n < 8; ++n) bA[512 * a2 + (t & 63) + 64 * n] = v[n];
  __syncthreads();
#pragma unroll
  for (int i = 0; i < 8; ++i) v[i] = bA[512 * i + t];
  dft8<1>(v);
}

static __device__ __forceinline__ void init_twiddles(cf* TW, int t) {
  const float R = 0.70710678118654752f;
  float sn, c;
  sincospif(-(float)t * (1.0f / 2048.0f), &sn, &c);
  const float a = (c + sn) * R, b = (sn - c) * R;
  TW[t] = cf{c, sn};
  TW[t + NT] = cf{a, b};
  TW[t + 2 * NT] = cf{sn, -c};
  TW[t + 3 * NT] = cf{b, -a};
  TW[t + 4 * NT] = cf{-c, -sn};
  TW[t + 5 * NT] = cf{-a, -b};
  TW[t + 6 * NT] = cf{-sn, c};
  TW[t + 7 * NT] = cf{-b, a};
}

static __device__ __forceinline__ cf cexp_l(float re, float im, float l) {
  float e = expf(re * l);
  float s, c;
  sincosf(im * l, &s, &c);
  return cf{e * c, e * s};
}

#define TZS 65
#define TWS 45
#define PREP_TAB_BYTES (128 * TZS * 8 + 128 * TWS * 8)
#define PREP_LDS (PREP_TAB_BYTES + 2 * 704 * 4)

__global__ __launch_bounds__(NT) void prep_kernel(
    const u16* __restrict__ Are, const u16* __restrict__ Aim, const u16* __restrict__ Bre, const u16* __restrict__ Bim,
    const u16* __restrict__ Cre, const u16* __restrict__ Cim, const u16* __restrict__ logdt,
    const u16* __restrict__ x0, const u16* __restrict__ Wg, const u16* __restrict__ Wu, const u16* __restrict__ Wd,
    float4* __restrict__ PQ, u16* __restrict__ W1f, u16* __restrict__ Wdf, float2* __restrict__ st0,
    float* __restrict__ csum, u16* __restrict__ x0k) {
  extern __shared__ __attribute__((aligned(16))) char smem[];
  const int t = threadIdx.x;
  const int p = blockIdx.x;
  cf* TZ = reinterpret_cast<cf*>(smem);
  cf* TWt = TZ + 128 * TZS;
  float* Kv = reinterpret_cast<float*>(smem + PREP_TAB_BYTES);
  __shared__ float csp[16][14];

  const int wave = t >> 6, lane = t & 63;
  const int ach = wave >> 2, grp = wave & 3;
  f32x4 kacc = f32x4{0.f, 0.f, 0.f, 0.f};

  if (t >= 256) {
    const int j = t - 256;
    u16 w1[4][8], wd[2][8], cv[32];
    uint4 xq[2];
#pragma unroll
    for (int i = 0; i < 4; ++i) {
      if (i < 3 || j < 128) {
        int u = p * 896 + j + 256 * i;
        int ln = u & 63, nt = (u >> 6) % 14, r = (u >> 6) / 14, ks = r & 15, cb = r >> 4;
        const u16* W = (nt < 7) ? Wg : Wu;
        int f = cb * 112 + (nt % 7) * 16 + (ln & 15);
        int k = ks * 32 + (ln >> 4) * 8;
#pragma unroll
        for (int e = 0; e < 8; ++e) w1[i][e] = W[(k + e) * FF + f];
      }
    }
#pragma unroll
    for (int i = 0; i < 2; ++i) {
      if (i < 1 || j < 192) {
        int u = p * 448 + j + 256 * i;
        int ln = u & 63, nt = (u >> 6) & 3, r = u >> 8, ks = r % 56, nb = r / 56;
        int n = nb * 64 + nt * 16 + (ln & 15);
        int k = ks * 32 + (ln >> 4) * 8;
#pragma unroll
        for (int e = 0; e < 8; ++e) wd[i][e] = Wd[(k + e) * DM + n];
      }
    }
    const int cpart = (j < 224) ? (j / 14) : 0, ccol = p * 14 + j % 14;
    {
      const u16* W = (ccol < FF) ? Wg : Wu;
      int f = (ccol < FF) ? ccol : ccol - FF;
#pragma unroll
      for (int k = 0; k < 32; ++k) cv[k] = W[(cpart * 32 + k) * FF + f];
    }
#pragma unroll
    for (int i = 0; i < 2; ++i) xq[i] = *reinterpret_cast<const uint4*>(x0 + (size_t)(p * 8 + (wave - 4) * 2 + i) * DM + lane * 8);
#pragma unroll
    for (int i = 0; i < 4; ++i) {
      if (i < 3 || j < 128) {
        int u = p * 896 + j + 256 * i;
        *reinterpret_cast<uint4*>(W1f + (size_t)u * 8) =
            make_uint4(w1[i][0] | ((unsigned)w1[i][1] << 16), w1[i][2] | ((unsigned)w1[i][3] << 16),
                       w1[i][4] | ((unsigned)w1[i][5] << 16), w1[i][6] | ((unsigned)w1[i][7] << 16));
      }
    }
#pragma unroll
    for (int i = 0; i < 2; ++i) {
      if (i < 1 || j < 192) {
        int u = p * 448 + j + 256 * i;
        *reinterpret_cast<uint4*>(Wdf + (size_t)u * 8) =
            make_uint4(wd[i][0] | ((unsigned)wd[i][1] << 16), wd[i][2] | ((unsigned)wd[i][3] << 16),
                       wd[i][4] | ((unsigned)wd[i][5] << 16), wd[i][6] | ((unsigned)wd[i][7] << 16));
      }
    }
    {
      float sacc = 0.f;
#pragma unroll
      for (int k = 0; k < 32; ++k) sacc += bf2f(cv[k]);
      if (j < 224) csp[cpart][j % 14] = sacc;
    }
#pragma unroll
    for (int i = 0; i < 2; ++i) {
      int row = p * 8 + (wave - 4) * 2 + i;
      *reinterpret_cast<uint4*>(x0k + ((size_t)lane * LSEQ + row) * 8) = xq[i];
      unsigned w[4] = {xq[i].x, xq[i].y, xq[i].z, xq[i].w};
      float s = 0.f, ss = 0.f;
#pragma unroll
      for (int e = 0; e < 4; ++e) {
        float a = bf2f((u16)(w[e] & 0xffff)), b = bf2f((u16)(w[e] >> 16));
        s += a + b; ss += a * a + b * b;
      }
#pragma unroll
      for (int o = 32; o > 0; o >>= 1) { s += __shfl_xor(s, o); ss += __shfl_xor(ss, o); }
      if (lane == 0) { st0[row] = make_float2(s, ss); st0[LSEQ + row] = make_float2(0.f, 0.f); st0[2 * LSEQ + row] = make_float2(0.f, 0.f); }
    }
  }

  for (int half = 0; half < 2; ++half) {
    if (t < 256) {
      const int slot = t & 127;
      const int d = 2 * p + (slot >> 6);
      const int n = half * 64 + (slot & 63);
      const float dt = rbf(expf(bf2f(logdt[d])));
      const float re = dt * bf2f(Are[d * NST + n]);
      const float im = dt * bf2f(Aim[d * NST + n]);
      if (t < 128) {
        cf zs = cexp_l(re, im, 1.0f);
#pragma unroll
        for (int s = 0; s < 4; ++s) {
          cf a{1.f, 0.f};
          TZ[slot * TZS + s * 16] = a;
#pragma unroll
          for (int b = 1; b < 16; ++b) { a = cmul(a, zs); TZ[slot * TZS + s * 16 + b] = a; }
          zs = cmul(zs, zs);
        }
      } else {
        cf dB{dt * bf2f(Bre[d * NST + n]), dt * bf2f(Bim[d * NST + n])};
        cf Cc{bf2f(Cre[d * NST + n]), bf2f(Cim[d * NST + n])};
        cf c = cmul(Cc, dB);
        cf z16 = cexp_l(re, im, 16.0f);
        cf z256 = cexp_l(re, im, 256.0f);
        cf a = c;
        TWt[slot * TWS + 0] = a;
#pragma unroll
        for (int i = 1; i < 16; ++i) { a = cmul(a, z16); TWt[slot * TWS + i] = a; }
        cf zs16 = cmul(z16, z16);
        cf zs128 = z256;
#pragma unroll
        for (int q = 0; q < 3; ++q) {
          a = cmul(c, zs128);
          TWt[slot * TWS + 16 + 8 * q] = a;
#pragma unroll
          for (int i = 1; i < 8; ++i) { a = cmul(a, zs16); TWt[slot * TWS + 16 + 8 * q + i] = a; }
          if (q == 2) { a = cmul(a, zs16); TWt[slot * TWS + 40] = a; }
          zs16 = cmul(zs16, zs16);
          zs128 = cmul(zs128, zs128);
        }
        TWt[slot * TWS + 41] = cf{0.f, 0.f};
        TWt[slot * TWS + 42] = cf{0.f, 0.f};
        TWt[slot * TWS + 43] = cf{0.f, 0.f};
      }
    }
    __syncthreads();
    {
      const int rbase = (grp == 0) ? 0 : (8 + 8 * grp);
      const float* pa = reinterpret_cast<const float*>(TWt) + ((ach * 64 + (lane >> 5)) * TWS + rbase + (lane & 15)) * 2 + ((lane >> 4) & 1);
      const float* pb = reinterpret_cast<const float*>(TZ) + ((ach * 64 + (lane >> 5)) * TZS + grp * 16 + (lane & 15)) * 2 + ((lane >> 4) & 1);
      const float sg = ((lane >> 4) & 1) ? -1.0f : 1.0f;
#pragma unroll 8
      for (int i = 0; i < 32; ++i) {
        float a = pa[i * (4 * TWS)] * sg;
        float b = pb[i * (4 * TZS)];
        kacc = __builtin_amdgcn_mfma_f32_16x16x4f32(a, b, kacc, 0, 0, 0);
      }
    }
    if (half == 0 && t >= 256 && t < 256 + 14) {
      float sacc = 0.f;
#pragma unroll
      for (int q = 0; q < 16; ++q) sacc += csp[q][t - 256];
      csum[p * 14 + (t - 256)] = sacc;
    }
    __syncthreads();
  }
  {
    const int rbase = (grp == 0) ? 0 : (8 + 8 * grp);
    const int nrow = (grp == 0) ? 16 : ((grp == 3) ? 12 : 8);
#pragma unroll
    for (int v2 = 0; v2 < 4; ++v2) {
      const int r = (lane >> 4) * 4 + v2;
      if (r < nrow) Kv[ach * 704 + (rbase + r) * 16 + (lane & 15)] = kacc[v2];
    }
  }
  __syncthreads();

  cf* bufA = reinterpret_cast<cf*>(smem);
  cf* bufB = bufA + XBUF;
  cf* TW = bufB + XBUF;
  init_twiddles(TW, t);
  cf v[8];
#pragma unroll
  for (int j = 0; j < 4; ++j) {
    int l = NT * j + t;
    unsigned ub = __float_as_uint((float)l);
    ub = (ub + 0x7fffu + ((ub >> 16) & 1u)) & 0xffff0000u;
    int lb = (int)__uint_as_float(ub);
    int row, b;
    if (lb < 256) { row = lb >> 4; b = lb & 15; }
    else {
      int q = (lb < 512) ? 0 : ((lb < 1024) ? 1 : 2);
      int vv = lb >> (q + 1);
      row = 16 + 8 * q + ((vv >> 4) - 8); b = vv & 15;
    }
    v[j] = cf{Kv[row * 16 + b], Kv[704 + row * 16 + b]};
  }
#pragma unroll
  for (int j = 4; j < 8; ++j) v[j] = cf{0.f, 0.f};
  __syncthreads();
  fft_fwd(v, bufA, bufB, TW, t);
#pragma unroll
  for (int k = 0; k < 8; ++k) bufB[t + NT * k] = v[k];
  __syncthreads();
  const float sc = 0.25f / 4096.0f;
#pragma unroll
  for (int k3 = 0; k3 < 8; ++k3) {
    int k = t + NT * k3;
    cf gg = v[k3];
    cf m = bufB[(4096 - k) & 4095];
    cf h{m.x, -m.y};
    cf P{(gg.x + gg.y) + (h.x - h.y), (gg.y - gg.x) + (h.y + h.x)};
    cf Q{(gg.x - gg.y) + (h.x + h.y), (gg.y + gg.x) + (h.y - h.x)};
    PQ[((size_t)p * 8 + k3) * NT + t] = make_float4(P.x * sc, P.y * sc, Q.x * sc, Q.y * sc);
  }
}

__global__ __launch_bounds__(NT) void conv_kernel(const u16* __restrict__ xin, const float2* __restrict__ stats,
                                                  const float4* __restrict__ PQ, const u16* __restrict__ Dskip,
                                                  u16* __restrict__ xout) {
  extern __shared__ __attribute__((aligned(16))) char smem[];
  cf* bufA = reinterpret_cast<cf*>(smem);
  cf* bufB = bufA + XBUF;
  cf* TW = bufB + XBUF;
  const int t = threadIdx.x, p = (blockIdx.x & 7) * 32 + (blockIdx.x >> 3);
  unsigned xr[4];
#pragma unroll
  for (int j = 0; j < 4; ++j)
    xr[j] = *reinterpret_cast<const unsigned*>(xin + ((size_t)(p >> 2) * LSEQ + NT * j + t) * 8 + (p & 3) * 2);
  float2 st[4];
#pragma unroll
  for (int j = 0; j < 4; ++j) st[j] = stats[NT * j + t];
  float4 pq[8];
#pragma unroll
  for (int k = 0; k < 8; ++k) pq[k] = PQ[((size_t)p * 8 + k) * NT + t];
  init_twiddles(TW, t);
  cf v[8];
  unsigned ur[4];
#pragma unroll
  for (int j = 0; j < 4; ++j) {
    const float s = st[j].x, ss = st[j].y;
    float mean = s * (1.0f / DM);
    float var = fmaxf(ss * (1.0f / DM) - mean * mean, 0.f);
    float m = rbf(mean);
    float sd = rbf(sqrtf(rbf(rbf(var) + 1e-5f)));
    float xa = bf2f((u16)(xr[j] & 0xffff)), xb = bf2f((u16)(xr[j] >> 16));
    float ua = rbf(rbf(xa - m) / sd), ub = rbf(rbf(xb - m) / sd);
    v[j] = cf{ua, ub};
    ur[j] = pack2(ua, ub);
  }
#pragma unroll
  for (int j = 4; j < 8; ++j) v[j] = cf{0.f, 0.f};
  __syncthreads();
  fft_fwd(v, bufA, bufB, TW, t);
#pragma unroll
  for (int k = 0; k < 8; ++k) bufB[t + NT * k] = v[k];
  __syncthreads();
#pragma unroll
  for (int k = 0; k < 8; ++k) {
    cf zc = bufB[(4096 - (t + NT * k)) & 4095];
    zc.y = -zc.y;
    cf a = cmul(v[k], cf{pq[k].x, pq[k].y});
    cf b = cmul(zc, cf{pq[k].z, pq[k].w});
    v[k] = cf{a.x + b.x, a.y + b.y};
  }
  fft_inv(v, bufA, bufB, TW, t);
  const float Da = bf2f(Dskip[2 * p]), Db = bf2f(Dskip[2 * p + 1]);
#pragma unroll
  for (int j = 0; j < 4; ++j) {
    float xa = bf2f((u16)(xr[j] & 0xffff)), xb = bf2f((u16)(xr[j] >> 16));
    float ua = bf2f((u16)(ur[j] & 0xffff)), ub = bf2f((u16)(ur[j] >> 16));
    float ya = rbf(rbf(v[j].x) + rbf(ua * Da));
    float yb = rbf(rbf(v[j].y) + rbf(ub * Db));
    *reinterpret_cast<unsigned*>(xout + ((size_t)(p >> 2) * LSEQ + NT * j + t) * 8 + (p & 3) * 2) = pack2(xa + ya, xb + yb);
  }
}

static __device__ __forceinline__ float gelu_tanh(float x) {
  float inner = 0.7978845608028654f * (x + 0.044715f * x * x * x);
  float e = __expf(2.0f * inner);
  float th = 1.0f - 2.0f * __builtin_amdgcn_rcpf(e + 1.0f);
  return 0.5f * x * (1.0f + th);
}

#define FA_NS 4
#define FA_STAGE_BYTES (28 * 1024)

__global__ __launch_bounds__(512) void ffn_a_kernel(const u16* __restrict__ xin, const u16* __restrict__ W1f,
                                                    const float* __restrict__ csum, u16* __restrict__ hf) {
  extern __shared__ __attribute__((aligned(16))) char smem[];
  __shared__ float2 rstat[4][2][32];
  const int t = threadIdx.x, wave = t >> 6, lane = t & 63;
  const int wm = wave >> 1, kh = wave & 1;
  const int bid = blockIdx.x;
  const int xcd = bid & 7, idx = bid >> 3;
  const int mblk = (xcd & 1) * 8 + (idx & 7);
  const int cb = (xcd >> 1) * 4 + (idx >> 3);
  const char* wsrc = reinterpret_cast<const char*>(W1f) + (size_t)cb * (16 * 14 * 1024);

#define FA_ISSUE(s)                                                                                   \
  do {                                                                                                \
    _Pragma("unroll") for (int i = 0; i < 4; ++i) {                                                  \
      const int fr = wave + 8 * i;                                                                    \
      if (i < 3 || wave < 4) {                                                                        \
        const int ksg = (fr < 14) ? (s) : (8 + (s));                                                  \
        const int nt = (fr < 14) ? fr : (fr - 14);                                                    \
        const char* g = wsrc + (size_t)(ksg * 14 + nt) * 1024 + lane * 16;                            \
        char* l = smem + ((s) % FA_NS) * FA_STAGE_BYTES + fr * 1024;                                  \
        __builtin_amdgcn_global_load_lds(reinterpret_cast<const unsigned*>(g),                        \
                                         reinterpret_cast<unsigned*>(l), 16, 0, 0);                   \
      }                                                                                               \
    }                                                                                                 \
  } while (0)
  FA_ISSUE(0); FA_ISSUE(1); FA_ISSUE(2);

  bf16x8 af[2][8];
  const int rbase = mblk * 128 + wm * 32 + (lane & 15);
#pragma unroll
  for (int mt = 0; mt < 2; ++mt)
#pragma unroll
    for (int ks = 0; ks < 8; ++ks)
      af[mt][ks] = *reinterpret_cast<const bf16x8*>(xin + ((size_t)((kh * 8 + ks) * 4 + (lane >> 4)) * LSEQ + rbase + mt * 16) * 8);
  __shared__ float cs_lds[224];
  if (t < 224) cs_lds[t] = csum[(t < 112) ? (cb * 112 + t) : (FF + cb * 112 + t - 112)];
  {
#pragma unroll
    for (int mt = 0; mt < 2; ++mt) {
      float s = 0.f, q = 0.f;
#pragma unroll
      for (int ks = 0; ks < 8; ++ks)
#pragma unroll
        for (int i = 0; i < 8; ++i) { float v = bf2f((u16)af[mt][ks][i]); s += v; q += v * v; }
      s += __shfl_xor(s, 16); s += __shfl_xor(s, 32);
      q += __shfl_xor(q, 16); q += __shfl_xor(q, 32);
      if (lane < 16) rstat[wm][kh][mt * 16 + lane] = make_float2(s, q);
    }
  }

  f32x4 acc[14][2];
#pragma unroll
  for (int nt = 0; nt < 14; ++nt) { acc[nt][0] = f32x4{0.f, 0.f, 0.f, 0.f}; acc[nt][1] = f32x4{0.f, 0.f, 0.f, 0.f}; }

#pragma unroll
  for (int it = 0; it < 8; ++it) {
    if (wave < 4) {
      if (it <= 5) asm volatile("s_waitcnt vmcnt(8)" ::: "memory");
      else if (it == 6) asm volatile("s_waitcnt vmcnt(4)" ::: "memory");
      else asm volatile("s_waitcnt vmcnt(0)" ::: "memory");
    } else {
      if (it <= 5) asm volatile("s_waitcnt vmcnt(6)" ::: "memory");
      else if (it == 6) asm volatile("s_waitcnt vmcnt(3)" ::: "memory");
      else asm volatile("s_waitcnt vmcnt(0)" ::: "memory");
    }
    __builtin_amdgcn_s_barrier();
    if (it + 3 < 8) FA_ISSUE(it + 3);
    const char* sb = smem + (it % FA_NS) * FA_STAGE_BYTES + kh * 14 * 1024 + lane * 16;
    bf16x8 bfr[14];
#pragma unroll
    for (int nt = 0; nt < 14; ++nt) bfr[nt] = *reinterpret_cast<const bf16x8*>(sb + nt * 1024);
    __builtin_amdgcn_sched_barrier(0);
#pragma unroll
    for (int nt = 0; nt < 14; ++nt) {
      acc[nt][0] = __builtin_amdgcn_mfma_f32_16x16x32_bf16(bfr[nt], af[0][it], acc[nt][0], 0, 0, 0);
      acc[nt][1] = __builtin_amdgcn_mfma_f32_16x16x32_bf16(bfr[nt], af[1][it], acc[nt][1], 0, 0, 0);
    }
  }
  __syncthreads();
  f32x4 fin[14];
  f32x4* xch = reinterpret_cast<f32x4*>(smem) + (size_t)wave * (14 * 64);
  if (kh == 0) {
#pragma unroll
    for (int nt = 0; nt < 14; ++nt) { fin[nt] = acc[nt][0]; xch[nt * 64 + lane] = acc[nt][1]; }
  } else {
#pragma unroll
    for (int nt = 0; nt < 14; ++nt) { fin[nt] = acc[nt][1]; xch[nt * 64 + lane] = acc[nt][0]; }
  }
  __syncthreads();
  const f32x4* xin2 = reinterpret_cast<const f32x4*>(smem) + (size_t)(wave ^ 1) * (14 * 64);
#pragma unroll
  for (int nt = 0; nt < 14; ++nt) fin[nt] += xin2[nt * 64 + lane];
  const int mt = kh;
  float2 sa = rstat[wm][0][mt * 16 + (lane & 15)], sb2 = rstat[wm][1][mt * 16 + (lane & 15)];
  const float mean = (sa.x + sb2.x) * (1.0f / DM);
  const float var = fmaxf((sa.y + sb2.y) * (1.0f / DM) - mean * mean, 0.f);
  const float m = rbf(mean);
  const float rs = 1.0f / rbf(sqrtf(rbf(rbf(var) + 1e-5f)));
  const int mtg = mblk * 8 + wm * 2 + mt;
#pragma unroll
  for (int nt = 0; nt < 7; ++nt) {
    const int fl = cb * 112 + nt * 16 + (lane >> 4) * 4;
    const float4 cg = *reinterpret_cast<const float4*>(&cs_lds[nt * 16 + (lane >> 4) * 4]);
    const float4 cu = *reinterpret_cast<const float4*>(&cs_lds[112 + nt * 16 + (lane >> 4) * 4]);
    float cgv[4] = {cg.x, cg.y, cg.z, cg.w}, cuv[4] = {cu.x, cu.y, cu.z, cu.w};
    float hv[4];
#pragma unroll
    for (int j = 0; j < 4; ++j) {
      float ga = fin[nt][j];
      float ua = fin[nt + 7][j];
      float g = rbf((ga - m * cgv[j]) * rs);
      float u = rbf((ua - m * cuv[j]) * rs);
      hv[j] = rbf(gelu_tanh(g)) * u;
    }
    const int ks2 = fl >> 5, kq = (fl >> 3) & 3;
    size_t off = ((size_t)(mtg * 56 + ks2) * 64 + (lane & 15) + 16 * kq) * 8 + ((lane >> 4) & 1) * 4;
    *reinterpret_cast<uint2*>(hf + off) = make_uint2(pack2(hv[0], hv[1]), pack2(hv[2], hv[3]));
  }
}

#define FB_PD 4
__global__ __launch_bounds__(512) void ffn_b_kernel(const u16* __restrict__ hf, const u16* __restrict__ Wdf,
                                                    const u16* __restrict__ xres, u16* __restrict__ xout,
                                                    float2* __restrict__ stout, int out_rowmajor) {
  extern __shared__ __attribute__((aligned(16))) char smem[];
  f32x4 (*red)[16][64] = reinterpret_cast<f32x4 (*)[16][64]>(smem);
  const int t = threadIdx.x, wave = t >> 6, lane = t & 63;
  const int bid = blockIdx.x;
  const int nb = (bid >> 3) & 7;
  const int mb = (bid & 7) * 4 + (bid >> 6);
  f32x4 acc[4][4];
#pragma unroll
  for (int a = 0; a < 4; ++a)
#pragma unroll
    for (int b = 0; b < 4; ++b) acc[a][b] = f32x4{0.f, 0.f, 0.f, 0.f};
  const int k0 = wave * 7;
  const bf16x8* wp = reinterpret_cast<const bf16x8*>(Wdf) + ((size_t)(nb * 56 + k0) * 4) * 64 + lane;
  const bf16x8* hp = reinterpret_cast<const bf16x8*>(hf) + ((size_t)mb * 4 * 56 + k0) * 64 + lane;
  bf16x8 wf[7][4], hfr[7][4];
#pragma unroll
  for (int ks = 0; ks < FB_PD; ++ks)
#pragma unroll
    for (int i = 0; i < 4; ++i) {
      wf[ks][i] = wp[((size_t)ks * 4 + i) * 64];
      hfr[ks][i] = hp[((size_t)i * 56 + ks) * 64];
    }
#pragma unroll
  for (int ks = 0; ks < 7; ++ks) {
    if (ks + FB_PD < 7) {
#pragma unroll
      for (int i = 0; i < 4; ++i) {
        wf[ks + FB_PD][i] = wp[((size_t)(ks + FB_PD) * 4 + i) * 64];
        hfr[ks + FB_PD][i] = hp[((size_t)i * 56 + ks + FB_PD) * 64];
      }
    }
    __builtin_amdgcn_sched_barrier(0);
#pragma unroll
    for (int nt = 0; nt < 4; ++nt)
#pragma unroll
      for (int mt = 0; mt < 4; ++mt)
        acc[nt][mt] = __builtin_amdgcn_mfma_f32_16x16x32_bf16(wf[ks][nt], hfr[ks][mt], acc[nt][mt], 0, 0, 0);
  }
#pragma unroll
  for (int nt = 0; nt < 4; ++nt)
#pragma unroll
    for (int mt = 0; mt < 4; ++mt) red[wave][nt * 4 + mt][lane] = acc[nt][mt];
  __syncthreads();
  const int mt = wave >> 1;
  const int row = mb * 64 + mt * 16 + (lane & 15);
  float s = 0.f, ss = 0.f;
#pragma unroll
  for (int q = 0; q < 2; ++q) {
    const int nt = (wave & 1) * 2 + q;
    const int r = nt * 4 + mt;
    const f32x4 o = ((red[0][r][lane] + red[1][r][lane]) + (red[2][r][lane] + red[3][r][lane])) +
                    ((red[4][r][lane] + red[5][r][lane]) + (red[6][r][lane] + red[7][r][lane]));
    const int n = nb * 64 + nt * 16 + (lane >> 4) * 4;
    const size_t koff = ((size_t)(n >> 3) * LSEQ + row) * 8 + (n & 7);
    uint2 xr = *reinterpret_cast<const uint2*>(xres + koff);
    float x0 = rbf(bf2f((u16)(xr.x & 0xffff)) + rbf(o[0]));
    float x1 = rbf(bf2f((u16)(xr.x >> 16)) + rbf(o[1]));
    float x2 = rbf(bf2f((u16)(xr.y & 0xffff)) + rbf(o[2]));
    float x3 = rbf(bf2f((u16)(xr.y >> 16)) + rbf(o[3]));
    s += (x0 + x1) + (x2 + x3);
    ss += (x0 * x0 + x1 * x1) + (x2 * x2 + x3 * x3);
    const size_t ooff = out_rowmajor ? ((size_t)row * DM + n) : koff;
    *reinterpret_cast<uint2*>(xout + ooff) = make_uint2(pack2(x0, x1), pack2(x2, x3));
  }
  s += __shfl_xor(s, 16); s += __shfl_xor(s, 32);
  ss += __shfl_xor(ss, 16); ss += __shfl_xor(ss, 32);
  if (lane < 16) {
    unsafeAtomicAdd(&stout[row].x, s);
    unsafeAtomicAdd(&stout[row].y, ss);
  }
}

extern "C" void kernel_launch(void* const* d_in, const int* in_sizes, int n_in,
                              void* d_out, int out_size, void* d_ws, size_t ws_size,
                              hipStream_t stream) {
  const u16* x0 = (const u16*)d_in[0];
  const u16* Are = (const u16*)d_in[1];
  const u16* Aim = (const u16*)d_in[2];
  const u16* Bre = (const u16*)d_in[3];
  const u16* Bim = (const u16*)d_in[4];
  const u16* Cre = (const u16*)d_in[5];
  const u16* Cim = (const u16*)d_in[6];
  const u16* logdt = (const u16*)d_in[7];
  const u16* Dskip = (const u16*)d_in[8];
  const u16* Wg = (const u16*)d_in[10];
  const u16* Wu = (const u16*)d_in[11];
  const u16* Wd = (const u16*)d_in[12];
  char* ws = (char*)d_ws;
  float4* PQ = (float4*)(ws + WS_PQ);
  u16* W1f = (u16*)(ws + WS_W1);
  u16* Wdf = (u16*)(ws + WS_WD);
  u16* hf = (u16*)(ws + WS_H);
  u16* x1 = (u16*)(ws + WS_X1);
  u16* x2 = (u16*)(ws + WS_X2);
  u16* x3 = (u16*)(ws + WS_X3);
  float2* st0 = (float2*)(ws + WS_ST0);
  float2* st2 = st0 + LSEQ;
  float2* st4 = st0 + 2 * LSEQ;
  float* csum = (float*)(ws + WS_CS);
  u16* out = (u16*)d_out;

  const int conv_lds = (2 * XBUF + 4096) * 8;
  hipFuncSetAttribute((const void*)prep_kernel, hipFuncAttributeMaxDynamicSharedMemorySize, PREP_LDS);
  hipFuncSetAttribute((const void*)conv_kernel, hipFuncAttributeMaxDynamicSharedMemorySize, conv_lds);
  hipFuncSetAttribute((const void*)ffn_b_kernel, hipFuncAttributeMaxDynamicSharedMemorySize, 131072);
  hipFuncSetAttribute((const void*)ffn_a_kernel, hipFuncAttributeMaxDynamicSharedMemorySize, FA_NS * FA_STAGE_BYTES);

  prep_kernel<<<256, NT, PREP_LDS, stream>>>(Are, Aim, Bre, Bim, Cre, Cim, logdt, x0, Wg, Wu, Wd, PQ, W1f, Wdf, st0, csum, x3);
  conv_kernel<<<256, NT, conv_lds, stream>>>(x3, st0, PQ, Dskip, x1);
  ffn_a_kernel<<<256, 512, FA_NS * FA_STAGE_BYTES, stream>>>(x1, W1f, csum, hf);
  ffn_b_kernel<<<256, 512, 131072, stream>>>(hf, Wdf, x1, x2, st2, 0);
  conv_kernel<<<256, NT, conv_lds, stream>>>(x2, st2, PQ, Dskip, x3);
  ffn_a_kernel<<<256, 512, FA_NS * FA_STAGE_BYTES, stream>>>(x3, W1f, csum, hf);
  ffn_b_kernel<<<256, 512, 131072, stream>>>(hf, Wdf, x3, out, st4, 1);
}
```
